```python
import torch

def _softmax(x, axis=-1):
    return torch.softmax(x, dim=axis)
def _gelu(x):
    return torch.nn.functional.gelu(x, approximate="tanh")
def _silu(x):
    return torch.nn.functional.silu(x)
def _sigmoid(x):
    return torch.sigmoid(x)
def _layernorm(x, axis=-1, eps=1e-5):
    m = x.mean(dim=axis, keepdim=True)
    v = x.var(dim=axis, keepdim=True, unbiased=False)
    return (x - m) / torch.sqrt(v + eps)
def _scatter_add(tgt, idx, src, axis=0):
    return tgt.clone().index_add_(axis, idx, src)
def _segment_sum(src, seg, n):
    out = torch.zeros((n,) + src.shape[1:], dtype=src.dtype)
    return out.index_add_(0, seg, src)
def _pad(x, pw):
    flat = tuple(v for pair in reversed(pw) for v in pair)
    return torch.nn.functional.pad(x, flat)

kernel_name = 'sequential_preLN__sliding_block_cached__dense_activation_mish__decode'

def setup_inputs():

    _w = (lambda a: (lambda t: t.to(torch.bfloat16) if t.is_floating_point() else t)(torch.as_tensor(a)))
    def _A_setup():
        B, Sq, D = 1, 1, 448
        Hq, Hkv, Sk, Dh = 16, 8, 8192, 256
        assert Hq % Hkv == 0, (Hq, Hkv)
        return {
            "x": _w((lambda *s: torch.randn(*s, dtype=torch.bfloat16) * 0.05)(B, Sq, D)),
            "k_cache": _w((lambda *s: torch.randn(*s, dtype=torch.bfloat16) * 1.6)(B, Hkv, Sk, Dh)),
            "v_cache": _w((lambda *s: torch.randn(*s, dtype=torch.bfloat16) * 0.4)(B, Hkv, Sk, Dh)),
            "wq": _w((lambda *s: torch.randn(*s, dtype=torch.bfloat16) * 1.6)(D, Hq * Dh) * (D ** -0.5)),
            "wo": _w((lambda *s: torch.randn(*s, dtype=torch.bfloat16) * 0.4)(Hq * Dh, D) * ((Hq * Dh) ** -0.5)),
        }
    _A_in = _A_setup()

    def _B_setup():
        B, S, D, F = 1, 1, 448, 1792
        return {
            "x": _w((lambda *s: torch.randn(*s, dtype=torch.bfloat16) * 0.05)(B, S, D)),
            "w1": _w((lambda *s: torch.randn(*s, dtype=torch.bfloat16) * 0.8)(D, F) * (D ** -0.5)),
            "b1": _w((lambda *s: torch.randn(*s, dtype=torch.bfloat16) * 0.2)(F)),
            "w2": _w((lambda *s: torch.randn(*s, dtype=torch.bfloat16) * 0.1)(F, D) * (F ** -0.5)),
        }
    _B_in = _B_setup()

    return {
        "A_x": _A_in["x"],
        "A_k_cache": _A_in["k_cache"],
        "A_v_cache": _A_in["v_cache"],
        "A_wq": _A_in["wq"],
        "A_wo": _A_in["wo"],
        "B_x": _B_in["x"],
        "B_w1": _B_in["w1"],
        "B_b1": _B_in["b1"],
        "B_w2": _B_in["w2"],
    }

def reference(A_x, A_k_cache, A_v_cache, A_wq, A_wo, B_x, B_w1, B_b1, B_w2):
    def _A_ref(x, k_cache, v_cache, wq, wo):
        B, Sq, D = x.shape
        Hq, Dh = 16, 256
        Hkv, Sk = k_cache.shape[1], k_cache.shape[2]
        W = min(4096, max(256, Sk // 4))
        rep = Hq // Hkv
        q = (lambda x, *d: x.permute(*d))((x @ wq).reshape(B, Sq, Hq, Dh), 0, 2, 1, 3)
        k = (lambda x, n, axis: torch.repeat_interleave(x, n, dim=axis))(k_cache[:, :, Sk - W:, :], rep, axis=1)
        v = (lambda x, n, axis: torch.repeat_interleave(x, n, dim=axis))(v_cache[:, :, Sk - W:, :], rep, axis=1)
        scores = torch.einsum("bhqd,bhkd->bhqk", q, k) * (Dh ** -0.5)
        p = _softmax(scores, axis=-1)
        o = torch.einsum("bhqk,bhkd->bhqd", p, v)
        return (lambda x, *d: x.permute(*d))(o, 0, 2, 1, 3).reshape(B, Sq, Hq * Dh) @ wo

    def _B_ref(x, w1, b1, w2):
        h = torch.einsum("bsd,df->bsf", x, w1) + b1
        sp = torch.log1p(torch.exp(-torch.abs(h))) + torch.where(h > 0, h, torch.zeros_like(h))
        act = h * torch.tanh(sp)
        return torch.einsum("bsf,fd->bsd", act, w2)

    x = A_x
    h = x + _A_ref(_layernorm(x, axis=-1), A_k_cache, A_v_cache, A_wq, A_wo)
    h2 = h + _B_ref(_layernorm(h, axis=-1), B_w1, B_b1, B_w2)
    return h2
```

```cpp
#include <hip/hip_runtime.h>
#include <stdint.h>

#define GAS __attribute__((address_space(1)))
typedef unsigned u32;
typedef unsigned long long u64;
typedef unsigned short u16;
typedef u32 u32x4 __attribute__((ext_vector_type(4)));
typedef float f32x4 __attribute__((ext_vector_type(4)));
typedef __bf16 bf16x8 __attribute__((ext_vector_type(8)));
typedef GAS u64 gu64;
typedef GAS u32 gu32;
#define RLX_AGENT __ATOMIC_RELAXED, __HIP_MEMORY_SCOPE_AGENT

#define STAMPS 0
#define KV_NT 1

constexpr int D = 448, HKV = 8, SK = 8192, DH = 256, WIN = 2048, FF = 1792, QD = 4096;
constexpr int NWG = 256, NT = 512;
constexpr int NRED = 56, NMLP = 112, MLP0 = 56;
constexpr int G_EPOCH = 0;
constexpr int G_Q = 64;
constexpr int G_ML = G_Q + 8 * 256;
constexpr int G_H = G_ML + 8 * 2 * 32 * 2;
constexpr int G_O = G_H + 512;
constexpr int G_HP = G_O + 8 * 32 * 32 * 16;
constexpr int G_OUT = G_HP + NRED * 256 * 8;
constexpr int G_END = G_OUT + NRED * NMLP * 8;
constexpr int G_STAMP = ((G_END + 4095) / 4096) * 4096;
constexpr int SPIN_CAP = 1 << 17;

template <int CTRL> __device__ __forceinline__ float dpp(float x) {
  return __builtin_bit_cast(float, __builtin_amdgcn_mov_dpp(__builtin_bit_cast(int, x), CTRL, 0xf, 0xf, true));
}
constexpr int XOR1 = 0xB1, XOR2 = 0x4E, XOR7 = 0x141, XOR8 = 0x128;
__device__ __forceinline__ float x16_sum(float x) {
  auto s = __builtin_amdgcn_permlane16_swap(__float_as_uint(x), __float_as_uint(x), false, false);
  return __uint_as_float(s[0]) + __uint_as_float(s[1]);
}
__device__ __forceinline__ float x32_sum(float x) {
  auto s = __builtin_amdgcn_permlane32_swap(__float_as_uint(x), __float_as_uint(x), false, false);
  return __uint_as_float(s[0]) + __uint_as_float(s[1]);
}
__device__ __forceinline__ float x16_max(float x) {
  auto s = __builtin_amdgcn_permlane16_swap(__float_as_uint(x), __float_as_uint(x), false, false);
  return fmaxf(__uint_as_float(s[0]), __uint_as_float(s[1]));
}
__device__ __forceinline__ float x32_max(float x) {
  auto s = __builtin_amdgcn_permlane32_swap(__float_as_uint(x), __float_as_uint(x), false, false);
  return fmaxf(__uint_as_float(s[0]), __uint_as_float(s[1]));
}
__device__ __forceinline__ float row_sum(float v) {
  v += dpp<XOR1>(v); v += dpp<XOR2>(v); v += dpp<XOR7>(v); v += dpp<XOR8>(v); return v;
}
__device__ __forceinline__ float row_max(float v) {
  v = fmaxf(v, dpp<XOR1>(v)); v = fmaxf(v, dpp<XOR2>(v)); v = fmaxf(v, dpp<XOR7>(v)); v = fmaxf(v, dpp<XOR8>(v)); return v;
}
__device__ __forceinline__ float wave_sum(float v) { return x32_sum(x16_sum(row_sum(v))); }
__device__ __forceinline__ float bf_lo(u32 v) { return __uint_as_float(v << 16); }
__device__ __forceinline__ float bf_hi(u32 v) { return __uint_as_float(v & 0xffff0000u); }
__device__ __forceinline__ u32 f2bf(float f) { return (u32)__builtin_bit_cast(u16, (__bf16)f); }
__device__ __forceinline__ float bfr(float f) { return (float)(__bf16)f; }
__device__ __forceinline__ u32x4 ld16(const u16* p) { return *(const GAS u32x4*)p; }
__device__ __forceinline__ u32x4 ld16nt(const u16* p) {
  return __builtin_nontemporal_load((const GAS u32x4*)p);
}
__device__ __forceinline__ void put(gu64* g, u32 tag, u32 v) { __hip_atomic_store(g, ((u64)tag << 32) | v, RLX_AGENT); }
__device__ __forceinline__ void putf(gu64* g, u32 tag, float v) { put(g, tag, __float_as_uint(v)); }
__device__ __forceinline__ u64 get(gu64* g) { return __hip_atomic_load(g, RLX_AGENT); }
#define BAR() do { asm volatile("s_waitcnt lgkmcnt(0)" ::: "memory"); __builtin_amdgcn_s_barrier(); asm volatile("" ::: "memory"); } while (0)
#define PIN() asm volatile("" ::: "memory")

__device__ __forceinline__ float treduce16(float (&x)[16], int lane) {
#define TR_STEP(HALF, CTRL, BIT) _Pragma("unroll") for (int h = 0; h < (HALF); ++h) { \
    const float lo_ = x[h], hi_ = x[h + (HALF)]; const float t1_ = lo_ + dpp<CTRL>(lo_), t2_ = hi_ + dpp<CTRL>(hi_); \
    x[h] = (lane & (BIT)) ? t2_ : t1_; }
  TR_STEP(8, XOR8, 8) TR_STEP(4, XOR7, 4) TR_STEP(2, XOR2, 2) TR_STEP(1, XOR1, 1)
#undef TR_STEP
  return x32_sum(x16_sum(x[0]));
}
__device__ __forceinline__ f32x4 mfma(u32x4 a, u32x4 b, f32x4 c) {
  return __builtin_amdgcn_mfma_f32_16x16x32_bf16(__builtin_bit_cast(bf16x8, a), __builtin_bit_cast(bf16x8, b), c, 0, 0, 0);
}
__device__ __forceinline__ u32x4 diag(u32 vv, const u32x4& msk) { return u32x4{vv & msk[0], vv & msk[1], vv & msk[2], vv & msk[3]}; }
template <int R> __device__ __forceinline__ u32 quad_bcast(u32 v) { return (u32)__builtin_amdgcn_mov_dpp((int)v, R * 0x55, 0xf, 0xf, true); }

#define STAMP(i)

__global__ void __launch_bounds__(NT, 2)
fused_block(const u16* __restrict__ x, const u16* __restrict__ kc, const u16* __restrict__ vc, const u16* __restrict__ wq,
            const u16* __restrict__ wo, const u16* __restrict__ w1, const u16* __restrict__ b1, const u16* __restrict__ w2,
            u16* __restrict__ out, u64* __restrict__ ws_) {
  __shared__ __attribute__((aligned(16))) float lds[4096];
  float* red = lds;
  float* st = lds + 128;
  float* csum = lds + 192;
  u32* qs = (u32*)(lds + 512);
  float* ow = lds + 1024;
  float* mlw = lds + 3072;
  gu64* ws = (gu64*)ws_;
  const int tid = threadIdx.x, lane = tid & 63, w = __builtin_amdgcn_readfirstlane(tid >> 6);
  const int bid = blockIdx.x, g = bid & 7, c = bid >> 3;
  const int l16 = lane & 15, lg = lane >> 4;
  STAMP(0);
  const u32 ep = (u32)get(ws + G_EPOCH);
  u32x4 xr = {0, 0, 0, 0};
  if (lane < 56) xr = ld16(x + lane * 8);
  const int row = (w < 7 ? w : 6) * 64 + lane;
  const u32 xo = x[row];
  const u16* wqp = wq + (size_t)row * QD + g * 512 + c * 16;
  const u32x4 wq0 = ld16(wqp), wq1 = ld16(wqp + 8);
  u32x4 kr[8], vr[8];
  if (w < 4) {
    const size_t key0 = (size_t)g * SK + (SK - WIN) + c * 64 + w * 16;
    const u16* kp = kc + (key0 + l16) * DH + lg * 8;
#pragma unroll
    for (int s = 0; s < 8; ++s) kr[s] = ld16nt(kp + s * 32);
    const u16* vp = vc + (key0 + lg * 4) * DH + l16 * 8;
#pragma unroll
    for (int i = 0; i < 4; ++i) { vr[2 * i] = ld16nt(vp + i * DH); vr[2 * i + 1] = ld16nt(vp + i * DH + 128); }
  }
  PIN();
  const u32 tag = ep * 0x9E3779B1u + 0x7F4A7C15u;
  u32x4 msk;
#pragma unroll
  for (int r = 0; r < 4; ++r) msk[r] = ((lane & 7) >> 1) == r ? ((lane & 1) ? 0xffff0000u : 0x0000ffffu) : 0u;
  {
    float xs[8];
#pragma unroll
    for (int k = 0; k < 4; ++k) { xs[2 * k] = bf_lo(xr[k]); xs[2 * k + 1] = bf_hi(xr[k]); }
    float s = 0.f;
#pragma unroll
    for (int k = 0; k < 8; ++k) s += xs[k];
    const float mean = wave_sum(s) * (1.f / D);
    float v = 0.f;
#pragma unroll
    for (int k = 0; k < 8; ++k) { const float d = xs[k] - mean; v += d * d; }
    if (lane >= 56) v = 0.f;
    const float var = wave_sum(v) * (1.f / D);
    const float den = bfr(sqrtf(bfr(bfr(var) + 1e-5f)));
    const float xn = bfr(bfr(bf_lo(xo) - bfr(mean)) / den);
    float a[16];
#pragma unroll
    for (int k = 0; k < 4; ++k) { a[2 * k] = xn * bf_lo(wq0[k]); a[2 * k + 1] = xn * bf_hi(wq0[k]); a[8 + 2 * k] = xn * bf_lo(wq1[k]); a[9 + 2 * k] = xn * bf_hi(wq1[k]); }
    const float qsum = treduce16(a, lane);
    if (lane < 16) red[l16 * 8 + w] = w < 7 ? qsum : 0.f;
  }
  STAMP(1);
  BAR();
  if (tid < 8) {
    const f32x4 a0 = *(const f32x4*)(red + tid * 16), a1 = *(const f32x4*)(red + tid * 16 + 4), b0 = *(const f32x4*)(red + tid * 16 + 8), b1_ = *(const f32x4*)(red + tid * 16 + 12);
    const float qa = ((a0[0] + a0[1]) + (a0[2] + a0[3])) + ((a1[0] + a1[1]) + (a1[2] + a1[3]));
    const float qb = ((b0[0] + b0[1]) + (b0[2] + b0[3])) + ((b1_[0] + b1_[1]) + (b1_[2] + b1_[3]));
    put(ws + G_Q + g * 256 + c * 8 + tid, tag, f2bf(qa) | (f2bf(qb) << 16));
  }
  STAMP(2);
  const bool is_mlp = bid >= MLP0 && bid < MLP0 + NMLP;
  const int mj = bid - MLP0;
  u32x4 wr[4], w2r[4], w1a = {0, 0, 0, 0}, w1b = {0, 0, 0, 0};
  const int ccol = (w - 4) * 128 + l16 * 8;
  const bool colok = ccol < D;
  if (w >= 4) {
    gu64* p = ws + G_Q + g * 256 + (tid - 256);
    u64 v;
    for (int spin = 0; spin < SPIN_CAP; ++spin) { v = get(p); if (__all((u32)(v >> 32) == tag)) break; }
    qs[tid - 256] = (u32)v;
    const u16* wp = wo + (size_t)(g * 512 + c * 16 + lg * 4) * D + (colok ? ccol : 0);
#pragma unroll
    for (int r = 0; r < 4; ++r) wr[r] = ld16nt(wp + r * D);
    if (is_mlp) {
      const u16* wp2 = w2 + (size_t)(mj * 16 + lg * 4) * D + (colok ? ccol : 0);
#pragma unroll
      for (int r = 0; r < 4; ++r) w2r[r] = ld16nt(wp2 + r * D);
    }
  }
  STAMP(3);
  BAR();
  if (w < 4) {
    f32x4 s0 = {0.f, 0.f, 0.f, 0.f}, s1 = {0.f, 0.f, 0.f, 0.f};
    const u32* qp = qs + (l16 >> 3) * 128 + lg * 4;
#pragma unroll
    for (int s = 0; s < 8; s += 2) { s0 = mfma(kr[s], *(const u32x4*)(qp + s * 16), s0); s1 = mfma(kr[s + 1], *(const u32x4*)(qp + s * 16 + 16), s1); }
    constexpr float C2 = 0.0625f * 1.4426950408889634f;
    float sc[4];
#pragma unroll
    for (int i = 0; i < 4; ++i) sc[i] = bfr(s0[i] + s1[i]) * C2;
    const float m = x32_max(x16_max(fmaxf(fmaxf(sc[0], sc[1]), fmaxf(sc[2], sc[3]))));
    float p[4];
#pragma unroll
    for (int i = 0; i < 4; ++i) p[i] = __builtin_amdgcn_exp2f(sc[i] - m);
    const float l = x32_sum(x16_sum((p[0] + p[1]) + (p[2] + p[3])));
    STAMP(4);
    f32x4 o0 = {0.f, 0.f, 0.f, 0.f}, o1 = {0.f, 0.f, 0.f, 0.f};
#pragma unroll
    for (int i = 0; i < 4; ++i) {
      const u32 pb = f2bf(p[i]);
      const u32x4 a = diag(pb | (pb << 16), msk);
      o0 = mfma(a, vr[2 * i], o0); o1 = mfma(a, vr[2 * i + 1], o1);
    }
    float* op = ow + w * 512 + (lane >> 5) * 256 + l16 * 8 + (lg & 1) * 4;
    *(f32x4*)op = o0; *(f32x4*)(op + 128) = o1;
    if (lane == 0 || lane == 8) { mlw[(lane >> 3) * 8 + w] = m; mlw[(lane >> 3) * 8 + 4 + w] = l; }
  }
  if (is_mlp && w < 7) {
    const u16* wp1 = w1 + (size_t)row * FF + mj * 16;
    w1a = ld16(wp1); w1b = ld16(wp1 + 8);
  }
  STAMP(5);
  BAR();
  {
    const int h = tid >> 8;
    const f32x4 mw = *(const f32x4*)(mlw + h * 8), lw = *(const f32x4*)(mlw + h * 8 + 4);
    const float M = fmaxf(fmaxf(mw[0], mw[1]), fmaxf(mw[2], mw[3]));
    float o = 0.f, L = 0.f;
#pragma unroll
    for (int i = 0; i < 4; ++i) { const float e = __builtin_amdgcn_exp2f(mw[i] - M); o += e * ow[i * 512 + tid]; L += e * lw[i]; }
    putf(ws + G_O + ((g * 32 + (tid >> 4)) * 32 + c) * 16 + l16, tag, o);
    if ((tid & 255) < 2) putf(ws + G_ML + ((g * 2 + h) * 32 + c) * 2 + (tid & 1), tag, (tid & 1) ? L : M);
  }
  STAMP(6);
  if (w >= 4) {
    const int t = tid - 256;
    gu64* po = ws + G_O + (g * 32 + c) * 512 + t;
    gu64* pm = ws + G_ML + (g * 2 + (c >> 4)) * 64;
    u64 va, vb, vm, vl, ma, mb;
    for (int spin = 0; spin < SPIN_CAP; ++spin) {
      va = get(po); vb = get(po + 256); vm = get(pm + (lane & 31) * 2); vl = get(pm + (lane & 31) * 2 + 1);
      ma = get(pm + (t >> 4) * 2); mb = get(pm + (16 + (t >> 4)) * 2);
      const bool ok = (u32)(va >> 32) == tag && (u32)(vb >> 32) == tag && (u32)(vm >> 32) == tag && (u32)(vl >> 32) == tag && (u32)(ma >> 32) == tag && (u32)(mb >> 32) == tag;
      if (__all(ok)) break;
    }
    STAMP(7);
    const float mp = __uint_as_float((u32)vm), lp = __uint_as_float((u32)vl);
    const float M = x16_max(row_max(mp));
    const float L = x16_sum(row_sum(__builtin_amdgcn_exp2f(mp - M) * lp));
    float o = __builtin_amdgcn_exp2f(__uint_as_float((u32)ma) - M) * __uint_as_float((u32)va) + __builtin_amdgcn_exp2f(__uint_as_float((u32)mb) - M) * __uint_as_float((u32)vb);
    o = x32_sum(x16_sum(o)) / L;
    if (lane < 16) red[l16 * 4 + (w - 4)] = o;
  }
  BAR();
  if (w >= 4) {
    const f32x4 r4 = *(const f32x4*)(red + (lg * 4 + (lane & 3)) * 4);
    const u32 ob = f2bf((r4[0] + r4[1]) + (r4[2] + r4[3]));
    const u32 oo = ob | (ob << 16);
    f32x4 acc = {0.f, 0.f, 0.f, 0.f};
    acc = mfma(diag(quad_bcast<0>(oo), msk), wr[0], acc);
    acc = mfma(diag(quad_bcast<1>(oo), msk), wr[1], acc);
    acc = mfma(diag(quad_bcast<2>(oo), msk), wr[2], acc);
    acc = mfma(diag(quad_bcast<3>(oo), msk), wr[3], acc);
    if (colok) {
      gu64* ph = ws + G_HP + ((size_t)(ccol >> 3) * 256 + bid) * 8 + (lg & 1) * 4 + (lane >> 5) * 2;
      putf(ph, tag, lane < 32 ? acc[0] : acc[2]); putf(ph + 1, tag, lane < 32 ? acc[1] : acc[3]);
    }
  }
  STAMP(8);
  float hval = 0.f;
  if (bid < NRED) {
    gu64* p = ws + G_HP + (size_t)bid * 2048 + tid;
    const u32 xh = x[bid * 8 + (tid & 7)];
    u64 v0, v1, v2, v3;
    for (int spin = 0; spin < SPIN_CAP; ++spin) {
      v0 = get(p); v1 = get(p + 512); v2 = get(p + 1024); v3 = get(p + 1536);
      const bool ok = (u32)(v0 >> 32) == tag && (u32)(v1 >> 32) == tag && (u32)(v2 >> 32) == tag && (u32)(v3 >> 32) == tag;
      if (__all(ok)) break;
    }
    STAMP(9);
    float s = (__uint_as_float((u32)v0) + __uint_as_float((u32)v1)) + (__uint_as_float((u32)v2) + __uint_as_float((u32)v3));
    s += dpp<XOR8>(s); s = x32_sum(x16_sum(s));
    float* red3 = lds + 256;
    if (lane < 8) red3[lane * 8 + w] = s;
    BAR();
    if (tid < 8) {
      const f32x4 a0 = *(const f32x4*)(red3 + tid * 8), a1 = *(const f32x4*)(red3 + tid * 8 + 4);
      const float at = ((a0[0] + a0[1]) + (a0[2] + a0[3])) + ((a1[0] + a1[1]) + (a1[2] + a1[3]));
      hval = bfr(bf_lo(xh) + bfr(at));
      putf(ws + G_H + bid * 8 + tid, tag, hval);
    }
    STAMP(10);
    gu64* pq = ws + G_OUT + (size_t)bid * (NMLP * 8) + (tid < 448 ? tid : 0);
    for (int spin = 0; spin < SPIN_CAP; ++spin) {
      v0 = get(pq); v1 = get(pq + 448);
      const bool ok = (u32)(v0 >> 32) == tag && (u32)(v1 >> 32) == tag;
      if (__all(ok)) break;
    }
    STAMP(13);
    s = tid < 448 ? __uint_as_float((u32)v0) + __uint_as_float((u32)v1) : 0.f;
    s += dpp<XOR8>(s); s = x32_sum(x16_sum(s));
    float* red4 = lds + 384;
    if (lane < 8) red4[lane * 8 + w] = s;
    BAR();
    if (tid < 8) {
      const f32x4 a0 = *(const f32x4*)(red4 + tid * 8), a1 = *(const f32x4*)(red4 + tid * 8 + 4);
      const float mt = ((a0[0] + a0[1]) + (a0[2] + a0[3])) + ((a1[0] + a1[1]) + (a1[2] + a1[3]));
      out[bid * 8 + tid] = (u16)f2bf(hval + bfr(mt));
      if (bid == 0 && tid == 0) put(ws + G_EPOCH, 0u, tag);
    }
    STAMP(14);
  } else if (is_mlp) {
    float wf[16];
#pragma unroll
    for (int k = 0; k < 4; ++k) { wf[2 * k] = bf_lo(w1a[k]); wf[2 * k + 1] = bf_hi(w1a[k]); wf[8 + 2 * k] = bf_lo(w1b[k]); wf[9 + 2 * k] = bf_hi(w1b[k]); }
    {
      float a[16];
#pragma unroll
      for (int k = 0; k < 16; ++k) a[k] = wf[k];
      const float cs = treduce16(a, lane);
      if (lane < 16) csum[l16 * 8 + w] = cs;
    }
    const int n = lg * 4 + (lane & 3);
    const float bias = bf_lo(b1[mj * 16 + n]);
    gu64* p = ws + G_H + row;
    u64 v;
    for (int spin = 0; spin < SPIN_CAP; ++spin) { v = get(p); if (__all((u32)(v >> 32) == tag)) break; }
    STAMP(11);
    const float hv = w < 7 ? __uint_as_float((u32)v) : 0.f;
    float a[16];
#pragma unroll
    for (int k = 0; k < 16; ++k) a[k] = hv * wf[k];
    const float sn = treduce16(a, lane);
    const float s1 = wave_sum(hv);
    const float dv = hv - s1 * (1.f / 64);
    const float m2 = wave_sum(dv * dv);
    float* red5 = lds + 3200;
    if (lane < 16) red5[l16 * 8 + w] = sn;
    if (lane == 0) { st[w] = s1; st[8 + w] = m2; }
    BAR();
    if (w >= 4) {
      const f32x4 a0 = *(const f32x4*)(red5 + n * 8), a1 = *(const f32x4*)(red5 + n * 8 + 4);
      const f32x4 c0 = *(const f32x4*)(csum + n * 8), c1 = *(const f32x4*)(csum + n * 8 + 4);
      const f32x4 s0 = *(const f32x4*)(st), s1v = *(const f32x4*)(st + 4), q0 = *(const f32x4*)(st + 8), q1 = *(const f32x4*)(st + 12);
      const float S = ((a0[0] + a0[1]) + (a0[2] + a0[3])) + ((a1[0] + a1[1]) + a1[2]);
      const float C = ((c0[0] + c0[1]) + (c0[2] + c0[3])) + ((c1[0] + c1[1]) + c1[2]);
      const float mean = (((s0[0] + s0[1]) + (s0[2] + s0[3])) + ((s1v[0] + s1v[1]) + s1v[2])) * (1.f / D);
      float M2 = ((q0[0] + q0[1]) + (q0[2] + q0[3])) + ((q1[0] + q1[1]) + q1[2]);
      float dd = 0.f;
#pragma unroll
      for (int k = 0; k < 4; ++k) { const float d = s0[k] * (1.f / 64) - mean; dd += d * d; }
#pragma unroll
      for (int k = 0; k < 3; ++k) { const float d = s1v[k] * (1.f / 64) - mean; dd += d * d; }
      M2 += 64.f * dd;
      const float rstd = __builtin_amdgcn_rsqf(M2 * (1.f / D) + 1e-5f);
      const float pre = bfr(bfr(rstd * (S - mean * C)) + bias);
      const float t = __builtin_amdgcn_exp2f(-fabsf(pre) * 1.4426950408889634f);
      const float num = pre > 0.f ? 1.f + 2.f * t : t * t + 2.f * t;
      const float dn = pre > 0.f ? num + 2.f * t * t : num + 2.f;
      const u32 ab = f2bf(pre * num / dn);
      const u32 aa = ab | (ab << 16);
      f32x4 acc = {0.f, 0.f, 0.f, 0.f};
      acc = mfma(diag(quad_bcast<0>(aa), msk), w2r[0], acc);
      acc = mfma(diag(quad_bcast<1>(aa), msk), w2r[1], acc);
      acc = mfma(diag(quad_bcast<2>(aa), msk), w2r[2], acc);
      acc = mfma(diag(quad_bcast<3>(aa), msk), w2r[3], acc);
      if (colok) {
        gu64* ph = ws + G_OUT + ((size_t)(ccol >> 3) * NMLP + mj) * 8 + (lg & 1) * 4 + (lane >> 5) * 2;
        putf(ph, tag, lane < 32 ? acc[0] : acc[2]); putf(ph + 1, tag, lane < 32 ? acc[1] : acc[3]);
      }
    }
    STAMP(12);
  }
}

extern "C" void kernel_launch(void* const* d_in, const int* in_sizes, int n_in, void* d_out, int out_size, void* d_ws, size_t ws_size,
                              hipStream_t stream) {
  fused_block<<<NWG, NT, 0, stream>>>((const u16*)d_in[0], (const u16*)d_in[1], (const u16*)d_in[2], (const u16*)d_in[3], (const u16*)d_in[4],
                                      (const u16*)d_in[6], (const u16*)d_in[7], (const u16*)d_in[8], (u16*)d_out, (u64*)d_ws);
}
```

```cpp
#define STAMPS 1
#define DIAG 0
#include <hip/hip_runtime.h>
#include <stdint.h>

#define GAS __attribute__((address_space(1)))
typedef unsigned u32;
typedef unsigned long long u64;
typedef unsigned short u16;
typedef u32 u32x4 __attribute__((ext_vector_type(4)));
typedef float f32x4 __attribute__((ext_vector_type(4)));
typedef __bf16 bf16x8 __attribute__((ext_vector_type(8)));
typedef GAS u64 gu64;
typedef GAS u32 gu32;
#define RLX_AGENT __ATOMIC_RELAXED, __HIP_MEMORY_SCOPE_AGENT

#define KV_NT 1

constexpr int D = 448, HKV = 8, SK = 8192, DH = 256, WIN = 2048, FF = 1792, QD = 4096;
constexpr int NWG = 256, NT = 512;
constexpr int NRED = 56, NMLP = 112, MLP0 = 56;
constexpr int G_EPOCH = 0;
constexpr int G_Q = 64;
constexpr int G_ML = G_Q + 8 * 256;
constexpr int G_H = G_ML + 8 * 2 * 32 * 2;
constexpr int G_O = G_H + 512;
constexpr int G_HP = G_O + 8 * 32 * 32 * 16;
constexpr int G_OUT = G_HP + NRED * 256 * 8;
constexpr int G_END = G_OUT + NRED * NMLP * 8;
constexpr int G_STAMP = ((G_END + 4095) / 4096) * 4096;
constexpr int SPIN_CAP = 1 << 17;

template <int CTRL> __device__ __forceinline__ float dpp(float x) {
  return __builtin_bit_cast(float, __builtin_amdgcn_mov_dpp(__builtin_bit_cast(int, x), CTRL, 0xf, 0xf, true));
}
constexpr int XOR1 = 0xB1, XOR2 = 0x4E, XOR7 = 0x141, XOR8 = 0x128;
__device__ __forceinline__ float x16_sum(float x) {
  auto s = __builtin_amdgcn_permlane16_swap(__float_as_uint(x), __float_as_uint(x), false, false);
  return __uint_as_float(s[0]) + __uint_as_float(s[1]);
}
__device__ __forceinline__ float x32_sum(float x) {
  auto s = __builtin_amdgcn_permlane32_swap(__float_as_uint(x), __float_as_uint(x), false, false);
  return __uint_as_float(s[0]) + __uint_as_float(s[1]);
}
__device__ __forceinline__ float x16_max(float x) {
  auto s = __builtin_amdgcn_permlane16_swap(__float_as_uint(x), __float_as_uint(x), false, false);
  return fmaxf(__uint_as_float(s[0]), __uint_as_float(s[1]));
}
__device__ __forceinline__ float x32_max(float x) {
  auto s = __builtin_amdgcn_permlane32_swap(__float_as_uint(x), __float_as_uint(x), false, false);
  return fmaxf(__uint_as_float(s[0]), __uint_as_float(s[1]));
}
__device__ __forceinline__ float row_sum(float v) {
  v += dpp<XOR1>(v); v += dpp<XOR2>(v); v += dpp<XOR7>(v); v += dpp<XOR8>(v); return v;
}
__device__ __forceinline__ float row_max(float v) {
  v = fmaxf(v, dpp<XOR1>(v)); v = fmaxf(v, dpp<XOR2>(v)); v = fmaxf(v, dpp<XOR7>(v)); v = fmaxf(v, dpp<XOR8>(v)); return v;
}
__device__ __forceinline__ float wave_sum(float v) { return x32_sum(x16_sum(row_sum(v))); }
__device__ __forceinline__ float bf_lo(u32 v) { return __uint_as_float(v << 16); }
__device__ __forceinline__ float bf_hi(u32 v) { return __uint_as_float(v & 0xffff0000u); }
__device__ __forceinline__ u32 f2bf(float f) { return (u32)__builtin_bit_cast(u16, (__bf16)f); }
__device__ __forceinline__ float bfr(float f) { return (float)(__bf16)f; }
__device__ __forceinline__ u32x4 ld16(const u16* p) { return *(const GAS u32x4*)p; }
__device__ __forceinline__ u32x4 ld16nt(const u16* p) {
  return __builtin_nontemporal_load((const GAS u32x4*)p);
}
__device__ __forceinline__ void put(gu64* g, u32 tag, u32 v) { __hip_atomic_store(g, ((u64)tag << 32) | v, RLX_AGENT); }
__device__ __forceinline__ void putf(gu64* g, u32 tag, float v) { put(g, tag, __float_as_uint(v)); }
__device__ __forceinline__ u64 get(gu64* g) { return __hip_atomic_load(g, RLX_AGENT); }
#define BAR() do { asm volatile("s_waitcnt lgkmcnt(0)" ::: "memory"); __builtin_amdgcn_s_barrier(); asm volatile("" ::: "memory"); } while (0)
#define PIN() asm volatile("" ::: "memory")

__device__ __forceinline__ float treduce16(float (&x)[16], int lane) {
#define TR_STEP(HALF, CTRL, BIT) _Pragma("unroll") for (int h = 0; h < (HALF); ++h) { \
    const float lo_ = x[h], hi_ = x[h + (HALF)]; const float t1_ = lo_ + dpp<CTRL>(lo_), t2_ = hi_ + dpp<CTRL>(hi_); \
    x[h] = (lane & (BIT)) ? t2_ : t1_; }
  TR_STEP(8, XOR8, 8) TR_STEP(4, XOR7, 4) TR_STEP(2, XOR2, 2) TR_STEP(1, XOR1, 1)
#undef TR_STEP
  return x32_sum(x16_sum(x[0]));
}
__device__ __forceinline__ f32x4 mfma(u32x4 a, u32x4 b, f32x4 c) {
  return __builtin_amdgcn_mfma_f32_16x16x32_bf16(__builtin_bit_cast(bf16x8, a), __builtin_bit_cast(bf16x8, b), c, 0, 0, 0);
}
__device__ __forceinline__ u32x4 diag(u32 vv, const u32x4& msk) { return u32x4{vv & msk[0], vv & msk[1], vv & msk[2], vv & msk[3]}; }
template <int R> __device__ __forceinline__ u32 quad_bcast(u32 v) { return (u32)__builtin_amdgcn_mov_dpp((int)v, R * 0x55, 0xf, 0xf, true); }

#define STAMP(i) do { __builtin_amdgcn_sched_barrier(0); asm volatile("s_memrealtime %0\n\ts_waitcnt lgkmcnt(0)" : "=s"(ts[i]) :: "memory"); __builtin_amdgcn_sched_barrier(0); } while (0)
constexpr int NSTAMP = 16;

__global__ void __launch_bounds__(NT, 2)
fused_block(const u16* __restrict__ x, const u16* __restrict__ kc, const u16* __restrict__ vc, const u16* __restrict__ wq,
            const u16* __restrict__ wo, const u16* __restrict__ w1, const u16* __restrict__ b1, const u16* __restrict__ w2,
            u16* __restrict__ out, u64* __restrict__ ws_) {
  __shared__ __attribute__((aligned(16))) float lds[4096];
  float* red = lds;
  float* st = lds + 128;
  float* csum = lds + 192;
  u32* qs = (u32*)(lds + 512);
  float* ow = lds + 1024;
  float* mlw = lds + 3072;
  gu64* ws = (gu64*)ws_;
  const int tid = threadIdx.x, lane = tid & 63, w = __builtin_amdgcn_readfirstlane(tid >> 6);
  const int bid = blockIdx.x, g = bid & 7, c = bid >> 3;
  const int l16 = lane & 15, lg = lane >> 4;
  u64 ts[NSTAMP];
#pragma unroll
  for (int i = 0; i < NSTAMP; ++i) ts[i] = 0;
  STAMP(0);
  const u32 ep = (u32)get(ws + G_EPOCH);
  u32x4 xr = {0, 0, 0, 0};
  if (lane < 56) xr = ld16(x + lane * 8);
  const int row = (w < 7 ? w : 6) * 64 + lane;
  const u32 xo = x[row];
  const u16* wqp = wq + (size_t)row * QD + g * 512 + c * 16;
  const u32x4 wq0 = ld16(wqp), wq1 = ld16(wqp + 8);
  u32x4 kr[8], vr[8];
  if (w < 4) {
    const size_t key0 = (size_t)g * SK + (SK - WIN) + c * 64 + w * 16;
    const u16* kp = kc + (key0 + l16) * DH + lg * 8;
#pragma unroll
    for (int s = 0; s < 8; ++s) kr[s] = ld16nt(kp + s * 32);
    const u16* vp = vc + (key0 + lg * 4) * DH + l16 * 8;
#pragma unroll
    for (int i = 0; i < 4; ++i) { vr[2 * i] = ld16nt(vp + i * DH); vr[2 * i + 1] = ld16nt(vp + i * DH + 128); }
  }
  PIN();
  const u32 tag = ep * 0x9E3779B1u + 0x7F4A7C15u;
  u32x4 msk;
#pragma unroll
  for (int r = 0; r < 4; ++r) msk[r] = ((lane & 7) >> 1) == r ? ((lane & 1) ? 0xffff0000u : 0x0000ffffu) : 0u;
  {
    float xs[8];
#pragma unroll
    for (int k = 0; k < 4; ++k) { xs[2 * k] = bf_lo(xr[k]); xs[2 * k + 1] = bf_hi(xr[k]); }
    float s = 0.f;
#pragma unroll
    for (int k = 0; k < 8; ++k) s += xs[k];
    const float mean = wave_sum(s) * (1.f / D);
    float v = 0.f;
#pragma unroll
    for (int k = 0; k < 8; ++k) { const float d = xs[k] - mean; v += d * d; }
    if (lane >= 56) v = 0.f;
    const float var = wave_sum(v) * (1.f / D);
    const float den = bfr(sqrtf(bfr(bfr(var) + 1e-5f)));
    const float xn = bfr(bfr(bf_lo(xo) - bfr(mean)) / den);
    float a[16];
#pragma unroll
    for (int k = 0; k < 4; ++k) { a[2 * k] = xn * bf_lo(wq0[k]); a[2 * k + 1] = xn * bf_hi(wq0[k]); a[8 + 2 * k] = xn * bf_lo(wq1[k]); a[9 + 2 * k] = xn * bf_hi(wq1[k]); }
    const float qsum = treduce16(a, lane);
    if (lane < 16) red[l16 * 8 + w] = w < 7 ? qsum : 0.f;
  }
  STAMP(1);
  BAR();
  if (tid < 8) {
    const f32x4 a0 = *(const f32x4*)(red + tid * 16), a1 = *(const f32x4*)(red + tid * 16 + 4), b0 = *(const f32x4*)(red + tid * 16 + 8), b1_ = *(const f32x4*)(red + tid * 16 + 12);
    const float qa = ((a0[0] + a0[1]) + (a0[2] + a0[3])) + ((a1[0] + a1[1]) + (a1[2] + a1[3]));
    const float qb = ((b0[0] + b0[1]) + (b0[2] + b0[3])) + ((b1_[0] + b1_[1]) + (b1_[2] + b1_[3]));
    put(ws + G_Q + g * 256 + c * 8 + tid, tag, f2bf(qa) | (f2bf(qb) << 16));
  }
  STAMP(2);
  const bool is_mlp = bid >= MLP0 && bid < MLP0 + NMLP;
  const int mj = bid - MLP0;
  u32x4 wr[4], w2r[4], w1a = {0, 0, 0, 0}, w1b = {0, 0, 0, 0};
  const int ccol = (w - 4) * 128 + l16 * 8;
  const bool colok = ccol < D;
  if (w >= 4) {
    gu64* p = ws + G_Q + g * 256 + (tid - 256);
    u64 v;
    for (int spin = 0; spin < SPIN_CAP; ++spin) { v = get(p); if (__all((u32)(v >> 32) == tag)) break; }
    qs[tid - 256] = (u32)v;
    const u16* wp = wo + (size_t)(g * 512 + c * 16 + lg * 4) * D + (colok ? ccol : 0);
#pragma unroll
    for (int r = 0; r < 4; ++r) wr[r] = ld16nt(wp + r * D);
    if (is_mlp) {
      const u16* wp2 = w2 + (size_t)(mj * 16 + lg * 4) * D + (colok ? ccol : 0);
#pragma unroll
      for (int r = 0; r < 4; ++r) w2r[r] = ld16nt(wp2 + r * D);
    }
  }
  STAMP(3);
  BAR();
  if (w < 4) {
    f32x4 s0 = {0.f, 0.f, 0.f, 0.f}, s1 = {0.f, 0.f, 0.f, 0.f};
    const u32* qp = qs + (l16 >> 3) * 128 + lg * 4;
#pragma unroll
    for (int s = 0; s < 8; s += 2) { s0 = mfma(kr[s], *(const u32x4*)(qp + s * 16), s0); s1 = mfma(kr[s + 1], *(const u32x4*)(qp + s * 16 + 16), s1); }
    constexpr float C2 = 0.0625f * 1.4426950408889634f;
    float sc[4];
#pragma unroll
    for (int i = 0; i < 4; ++i) sc[i] = bfr(s0[i] + s1[i]) * C2;
    const float m = x32_max(x16_max(fmaxf(fmaxf(sc[0], sc[1]), fmaxf(sc[2], sc[3]))));
    float p[4];
#pragma unroll
    for (int i = 0; i < 4; ++i) p[i] = __builtin_amdgcn_exp2f(sc[i] - m);
    const float l = x32_sum(x16_sum((p[0] + p[1]) + (p[2] + p[3])));
    STAMP(4);
    f32x4 o0 = {0.f, 0.f, 0.f, 0.f}, o1 = {0.f, 0.f, 0.f, 0.f};
#pragma unroll
    for (int i = 0; i < 4; ++i) {
      const u32 pb = f2bf(p[i]);
      const u32x4 a = diag(pb | (pb << 16), msk);
      o0 = mfma(a, vr[2 * i], o0); o1 = mfma(a, vr[2 * i + 1], o1);
    }
    float* op = ow + w * 512 + (lane >> 5) * 256 + l16 * 8 + (lg & 1) * 4;
    *(f32x4*)op = o0; *(f32x4*)(op + 128) = o1;
    if (lane == 0 || lane == 8) { mlw[(lane >> 3) * 8 + w] = m; mlw[(lane >> 3) * 8 + 4 + w] = l; }
  }
  if (is_mlp && w < 7) {
    const u16* wp1 = w1 + (size_t)row * FF + mj * 16;
    w1a = ld16(wp1); w1b = ld16(wp1 + 8);
  }
  STAMP(5);
  BAR();
  {
    const int h = tid >> 8;
    const f32x4 mw = *(const f32x4*)(mlw + h * 8), lw = *(const f32x4*)(mlw + h * 8 + 4);
    const float M = fmaxf(fmaxf(mw[0], mw[1]), fmaxf(mw[2], mw[3]));
    float o = 0.f, L = 0.f;
#pragma unroll
    for (int i = 0; i < 4; ++i) { const float e = __builtin_amdgcn_exp2f(mw[i] - M); o += e * ow[i * 512 + tid]; L += e * lw[i]; }
    putf(ws + G_O + ((g * 32 + (tid >> 4)) * 32 + c) * 16 + l16, tag, o);
    if ((tid & 255) < 2) putf(ws + G_ML + ((g * 2 + h) * 32 + c) * 2 + (tid & 1), tag, (tid & 1) ? L : M);
  }
  STAMP(6);
  if (w >= 4) {
    const int t = tid - 256;
    gu64* po = ws + G_O + (g * 32 + c) * 512 + t;
    gu64* pm = ws + G_ML + (g * 2 + (c >> 4)) * 64;
    u64 va, vb, vm, vl, ma, mb;
    for (int spin = 0; spin < SPIN_CAP; ++spin) {
      va = get(po); vb = get(po + 256); vm = get(pm + (lane & 31) * 2); vl = get(pm + (lane & 31) * 2 + 1);
      ma = get(pm + (t >> 4) * 2); mb = get(pm + (16 + (t >> 4)) * 2);
      const bool ok = (u32)(va >> 32) == tag && (u32)(vb >> 32) == tag && (u32)(vm >> 32) == tag && (u32)(vl >> 32) == tag && (u32)(ma >> 32) == tag && (u32)(mb >> 32) == tag;
      if (__all(ok)) break;
    }
    STAMP(7);
    const float mp = __uint_as_float((u32)vm), lp = __uint_as_float((u32)vl);
    const float M = x16_max(row_max(mp));
    const float L = x16_sum(row_sum(__builtin_amdgcn_exp2f(mp - M) * lp));
    float o = __builtin_amdgcn_exp2f(__uint_as_float((u32)ma) - M) * __uint_as_float((u32)va) + __builtin_amdgcn_exp2f(__uint_as_float((u32)mb) - M) * __uint_as_float((u32)vb);
    o = x32_sum(x16_sum(o)) / L;
    if (lane < 16) red[l16 * 4 + (w - 4)] = o;
  }
  BAR();
  if (w >= 4) {
    const f32x4 r4 = *(const f32x4*)(red + (lg * 4 + (lane & 3)) * 4);
    const u32 ob = f2bf((r4[0] + r4[1]) + (r4[2] + r4[3]));
    const u32 oo = ob | (ob << 16);
    f32x4 acc = {0.f, 0.f, 0.f, 0.f};
    acc = mfma(diag(quad_bcast<0>(oo), msk), wr[0], acc);
    acc = mfma(diag(quad_bcast<1>(oo), msk), wr[1], acc);
    acc = mfma(diag(quad_bcast<2>(oo), msk), wr[2], acc);
    acc = mfma(diag(quad_bcast<3>(oo), msk), wr[3], acc);
    if (colok) {
      gu64* ph = ws + G_HP + ((size_t)(ccol >> 3) * 256 + bid) * 8 + (lg & 1) * 4 + (lane >> 5) * 2;
      putf(ph, tag, lane < 32 ? acc[0] : acc[2]); putf(ph + 1, tag, lane < 32 ? acc[1] : acc[3]);
    }
  }
  STAMP(8);
  float hval = 0.f;
  if (bid < NRED) {
    gu64* p = ws + G_HP + (size_t)bid * 2048 + tid;
    const u32 xh = x[bid * 8 + (tid & 7)];
    u64 v0, v1, v2, v3;
    for (int spin = 0; spin < SPIN_CAP; ++spin) {
      v0 = get(p); v1 = get(p + 512); v2 = get(p + 1024); v3 = get(p + 1536);
      const bool ok = (u32)(v0 >> 32) == tag && (u32)(v1 >> 32) == tag && (u32)(v2 >> 32) == tag && (u32)(v3 >> 32) == tag;
      if (__all(ok)) break;
    }
    STAMP(9);
    float s = (__uint_as_float((u32)v0) + __uint_as_float((u32)v1)) + (__uint_as_float((u32)v2) + __uint_as_float((u32)v3));
    s += dpp<XOR8>(s); s = x32_sum(x16_sum(s));
    float* red3 = lds + 256;
    if (lane < 8) red3[lane * 8 + w] = s;
    BAR();
    if (tid < 8) {
      const f32x4 a0 = *(const f32x4*)(red3 + tid * 8), a1 = *(const f32x4*)(red3 + tid * 8 + 4);
      const float at = ((a0[0] + a0[1]) + (a0[2] + a0[3])) + ((a1[0] + a1[1]) + (a1[2] + a1[3]));
      hval = bfr(bf_lo(xh) + bfr(at));
      putf(ws + G_H + bid * 8 + tid, tag, hval);
    }
    STAMP(10);
    gu64* pq = ws + G_OUT + (size_t)bid * (NMLP * 8) + (tid < 448 ? tid : 0);
    for (int spin = 0; spin < SPIN_CAP; ++spin) {
      v0 = get(pq); v1 = get(pq + 448);
      const bool ok = (u32)(v0 >> 32) == tag && (u32)(v1 >> 32) == tag;
      if (__all(ok)) break;
    }
    STAMP(13);
    s = tid < 448 ? __uint_as_float((u32)v0) + __uint_as_float((u32)v1) : 0.f;
    s += dpp<XOR8>(s); s = x32_sum(x16_sum(s));
    float* red4 = lds + 384;
    if (lane < 8) red4[lane * 8 + w] = s;
    BAR();
    if (tid < 8) {
      const f32x4 a0 = *(const f32x4*)(red4 + tid * 8), a1 = *(const f32x4*)(red4 + tid * 8 + 4);
      const float mt = ((a0[0] + a0[1]) + (a0[2] + a0[3])) + ((a1[0] + a1[1]) + (a1[2] + a1[3]));
      out[bid * 8 + tid] = (u16)f2bf(hval + bfr(mt));
      if (bid == 0 && tid == 0) put(ws + G_EPOCH, 0u, tag);
    }
    STAMP(14);
  } else if (is_mlp) {
    float wf[16];
#pragma unroll
    for (int k = 0; k < 4; ++k) { wf[2 * k] = bf_lo(w1a[k]); wf[2 * k + 1] = bf_hi(w1a[k]); wf[8 + 2 * k] = bf_lo(w1b[k]); wf[9 + 2 * k] = bf_hi(w1b[k]); }
    {
      float a[16];
#pragma unroll
      for (int k = 0; k < 16; ++k) a[k] = wf[k];
      const float cs = treduce16(a, lane);
      if (lane < 16) csum[l16 * 8 + w] = cs;
    }
    const int n = lg * 4 + (lane & 3);
    const float bias = bf_lo(b1[mj * 16 + n]);
    gu64* p = ws + G_H + row;
    u64 v;
    for (int spin = 0; spin < SPIN_CAP; ++spin) { v = get(p); if (__all((u32)(v >> 32) == tag)) break; }
    STAMP(11);
    const float hv = w < 7 ? __uint_as_float((u32)v) : 0.f;
    float a[16];
#pragma unroll
    for (int k = 0; k < 16; ++k) a[k] = hv * wf[k];
    const float sn = treduce16(a, lane);
    const float s1 = wave_sum(hv);
    const float dv = hv - s1 * (1.f / 64);
    const float m2 = wave_sum(dv * dv);
    float* red5 = lds + 3200;
    if (lane < 16) red5[l16 * 8 + w] = sn;
    if (lane == 0) { st[w] = s1; st[8 + w] = m2; }
    BAR();
    if (w >= 4) {
      const f32x4 a0 = *(const f32x4*)(red5 + n * 8), a1 = *(const f32x4*)(red5 + n * 8 + 4);
      const f32x4 c0 = *(const f32x4*)(csum + n * 8), c1 = *(const f32x4*)(csum + n * 8 + 4);
      const f32x4 s0 = *(const f32x4*)(st), s1v = *(const f32x4*)(st + 4), q0 = *(const f32x4*)(st + 8), q1 = *(const f32x4*)(st + 12);
      const float S = ((a0[0] + a0[1]) + (a0[2] + a0[3])) + ((a1[0] + a1[1]) + a1[2]);
      const float C = ((c0[0] + c0[1]) + (c0[2] + c0[3])) + ((c1[0] + c1[1]) + c1[2]);
      const float mean = (((s0[0] + s0[1]) + (s0[2] + s0[3])) + ((s1v[0] + s1v[1]) + s1v[2])) * (1.f / D);
      float M2 = ((q0[0] + q0[1]) + (q0[2] + q0[3])) + ((q1[0] + q1[1]) + q1[2]);
      float dd = 0.f;
#pragma unroll
      for (int k = 0; k < 4; ++k) { const float d = s0[k] * (1.f / 64) - mean; dd += d * d; }
#pragma unroll
      for (int k = 0; k < 3; ++k) { const float d = s1v[k] * (1.f / 64) - mean; dd += d * d; }
      M2 += 64.f * dd;
      const float rstd = __builtin_amdgcn_rsqf(M2 * (1.f / D) + 1e-5f);
      const float pre = bfr(bfr(rstd * (S - mean * C)) + bias);
      const float t = __builtin_amdgcn_exp2f(-fabsf(pre) * 1.4426950408889634f);
      const float num = pre > 0.f ? 1.f + 2.f * t : t * t + 2.f * t;
      const float dn = pre > 0.f ? num + 2.f * t * t : num + 2.f;
      const u32 ab = f2bf(pre * num / dn);
      const u32 aa = ab | (ab << 16);
      f32x4 acc = {0.f, 0.f, 0.f, 0.f};
      acc = mfma(diag(quad_bcast<0>(aa), msk), w2r[0], acc);
      acc = mfma(diag(quad_bcast<1>(aa), msk), w2r[1], acc);
      acc = mfma(diag(quad_bcast<2>(aa), msk), w2r[2], acc);
      acc = mfma(diag(quad_bcast<3>(aa), msk), w2r[3], acc);
      if (colok) {
        gu64* ph = ws + G_OUT + ((size_t)(ccol >> 3) * NMLP + mj) * 8 + (lg & 1) * 4 + (lane >> 5) * 2;
        putf(ph, tag, lane < 32 ? acc[0] : acc[2]); putf(ph + 1, tag, lane < 32 ? acc[1] : acc[3]);
      }
    }
    STAMP(12);
  }
  if (bid == 0 && w == 0) { const u64 te = __builtin_amdgcn_s_memrealtime(), dd = ts[DIAG] - ts[0]; while (__builtin_amdgcn_s_memrealtime() - te < dd && dd < 10000) {} }
}

extern "C" void kernel_launch(void* const* d_in, const int* in_sizes, int n_in, void* d_out, int out_size, void* d_ws, size_t ws_size,
                              hipStream_t stream) {
  fused_block<<<NWG, NT, 0, stream>>>((const u16*)d_in[0], (const u16*)d_in[1], (const u16*)d_in[2], (const u16*)d_in[3], (const u16*)d_in[4],
                                      (const u16*)d_in[6], (const u16*)d_in[7], (const u16*)d_in[8], (u16*)d_out, (u64*)d_ws);
}
```

```cpp
#include <hip/hip_runtime.h>
#include <stdint.h>

#define GAS __attribute__((address_space(1)))
#define LAS __attribute__((address_space(3)))
typedef unsigned u32;
typedef unsigned long long u64;
typedef unsigned short u16;
typedef u32 u32x2 __attribute__((ext_vector_type(2)));
typedef u32 u32x3 __attribute__((ext_vector_type(3)));
typedef u32 u32x4 __attribute__((ext_vector_type(4)));
typedef u32 u32x4a __attribute__((ext_vector_type(4), aligned(4)));
typedef u32 u32x3a __attribute__((ext_vector_type(3), aligned(4)));
typedef float f32x2 __attribute__((ext_vector_type(2)));
typedef float f32x4 __attribute__((ext_vector_type(4)));
typedef __bf16 bf16x2 __attribute__((ext_vector_type(2)));
typedef __bf16 bf16x8 __attribute__((ext_vector_type(8)));
typedef __amdgpu_buffer_rsrc_t rsrc_t;

#define KV_NT 1
#define V_EARLY 2
#define D_K 0
#define D_V 2
#define D_Q 12
#define D_HP 8
#define D_OUT 20
#define EP_AUX 0
#define XROT4 ((c ^ (g * 2)) & 15)
#define XROT3 0
#define V_FIRST 1
#define D_WO 4
#define K_EARLY 4
#define D_W1 0
#define T_O 360
#define T_HP (T_O + 121)
#define T_H (T_HP + 84)
#define D_HR 10
#define WO_EW 2
#define WO_B3 1
#define T_ML 30
#define T_OUT (T_H + 105)
#define LD_AUX 16
#define ST_AUX 16
#define CODE_PF 13568
#define K_MID 8
#define WO_Q 1

constexpr int D = 448, SK = 8192, DH = 256, WIN = 2048, FF = 1792, QD = 4096;
constexpr int NWG = 256, NT = 512;
constexpr int NRED = 56, NCON = 128, NMLP = 112, MLP0 = 128;
constexpr int B_EPOCH = 0;
constexpr int B_Q = 256;
constexpr int QC = 8 * 8192;
constexpr int B_ML = B_Q + 3 * QC;
constexpr int B_H = B_ML + 8 * 2 * 32 * 16;
constexpr int B_T = B_H + 9 * 2048;
constexpr int B_O = B_T + 2048;
constexpr int B_HP = B_O + 8 * 32 * 2048;
constexpr int B_OUT = B_HP + 60 * NCON * 32;
constexpr int B_END = B_OUT + 60 * NMLP * 32;
constexpr int SPIN_CAP = 1 << 17;
constexpr int OOR = 0x7ffffff0;

template <int CTRL> __device__ __forceinline__ float dpp(float x) {
  return __builtin_bit_cast(float, __builtin_amdgcn_mov_dpp(__builtin_bit_cast(int, x), CTRL, 0xf, 0xf, true));
}
constexpr int XOR1 = 0xB1, XOR2 = 0x4E, XOR7 = 0x141, ROR4 = 0x124, ROR8 = 0x128;
__device__ __forceinline__ float asf(u32 v) { return __uint_as_float(v); }
__device__ __forceinline__ u32 asu(float v) { return __float_as_uint(v); }
__device__ __forceinline__ float x16_sum(float a, float b) { auto s = __builtin_amdgcn_permlane16_swap(asu(a), asu(b), false, false); return asf(s[0]) + asf(s[1]); }
__device__ __forceinline__ float x32_sum(float a, float b) { auto s = __builtin_amdgcn_permlane32_swap(asu(a), asu(b), false, false); return asf(s[0]) + asf(s[1]); }
__device__ __forceinline__ float x16_max(float x) { auto s = __builtin_amdgcn_permlane16_swap(asu(x), asu(x), false, false); return fmaxf(asf(s[0]), asf(s[1])); }
__device__ __forceinline__ float x32_max(float x) { auto s = __builtin_amdgcn_permlane32_swap(asu(x), asu(x), false, false); return fmaxf(asf(s[0]), asf(s[1])); }
__device__ __forceinline__ float row_sum(float v) { v += dpp<XOR1>(v); v += dpp<XOR2>(v); v += dpp<XOR7>(v); v += dpp<ROR8>(v); return v; }
__device__ __forceinline__ float row_max(float v) {
  v = fmaxf(v, dpp<XOR1>(v)); v = fmaxf(v, dpp<XOR2>(v)); v = fmaxf(v, dpp<XOR7>(v)); v = fmaxf(v, dpp<ROR8>(v)); return v;
}
__device__ __forceinline__ float wave_sum(float v) { v = row_sum(v); v = x16_sum(v, v); return x32_sum(v, v); }
__device__ __forceinline__ float bf_lo(u32 v) { return asf(v << 16); }
__device__ __forceinline__ float bf_hi(u32 v) { return asf(v & 0xffff0000u); }
__device__ __forceinline__ u32 pk(float lo, float hi) { return __builtin_bit_cast(u32, __builtin_convertvector(f32x2{lo, hi}, bf16x2)); }
__device__ __forceinline__ float bfr(float f) { return (float)(__bf16)f; }
__device__ __forceinline__ u32x4 ld16(const u16* p) { return *(const GAS u32x4*)p; }
__device__ __forceinline__ u32x4 ld16nt(const u16* p) {
  return __builtin_nontemporal_load((const GAS u32x4*)p);
}
__device__ __forceinline__ void put1(rsrc_t r, int off, u32 tag, u32 a) { __builtin_amdgcn_raw_buffer_store_b64(u32x2{a, tag}, r, off, 0, ST_AUX); }
__device__ __forceinline__ void put2(rsrc_t r, int off, u32 tag, u32 a, u32 b) { __builtin_amdgcn_raw_buffer_store_b128(u32x4{a, tag, b, tag}, r, off, 0, ST_AUX); }
__device__ __forceinline__ void get(rsrc_t r, int off, u32x4& v) { v = __builtin_amdgcn_raw_buffer_load_b128(r, off, 0, LD_AUX); }
__device__ __forceinline__ void get(rsrc_t r, int off, u32x2& v) { v = __builtin_amdgcn_raw_buffer_load_b64(r, off, 0, LD_AUX); }
__device__ __forceinline__ bool tagok(const u32x4& v, u32 tag) { return (v[1] == tag) & (v[3] == tag); }
__device__ __forceinline__ bool tagok(const u32x2& v, u32 tag) { return v[1] == tag; }
#define BAR() do { asm volatile("s_waitcnt lgkmcnt(0)" ::: "memory"); __builtin_amdgcn_s_barrier(); asm volatile("" ::: "memory"); } while (0)
#define PIN() asm volatile("" ::: "memory")
#define KEEP(v) asm volatile("" : "+v"(v))
#define SB() __builtin_amdgcn_sched_barrier(0)
template <int N> __device__ __forceinline__ void nap() { if (N > 0) { SB(); __builtin_amdgcn_s_sleep(N); SB(); } }
template <int N, typename V> __device__ __forceinline__ void poll_issue(rsrc_t r, const int (&off)[N], V (&v)[N]) {
  PIN();
#pragma unroll
  for (int k = 0; k < N; ++k) get(r, off[k], v[k]);
  SB();
}
template <int N, typename V, typename F> __device__ __forceinline__ void poll_wait(rsrc_t r, const int (&off)[N], const u32 (&tg)[N], V (&v)[N], F work) {
  work();
  bool all = true;
#pragma unroll
  for (int k = 0; k < N; ++k) all &= tagok(v[k], tg[k]);
  if (__builtin_expect(!__all(all), 0)) {
#pragma nounroll
    for (int spin = 0; spin < SPIN_CAP; ++spin) {
      PIN();
      V t[N];
#pragma unroll
      for (int k = 0; k < N; ++k) get(r, tagok(v[k], tg[k]) ? OOR : off[k], t[k]);
      SB();
      all = true;
#pragma unroll
      for (int k = 0; k < N; ++k) { const bool ok = tagok(v[k], tg[k]); v[k] = ok ? v[k] : t[k]; all &= ok | tagok(t[k], tg[k]); }
      if (__all(all)) break;
    }
    work();
  }
  PIN();
}
template <int N, typename V> __device__ __forceinline__ void poll_wait(rsrc_t r, const int (&off)[N], const u32 (&tg)[N], V (&v)[N]) {
  poll_wait<N>(r, off, tg, v, [] {});
}

__device__ __forceinline__ f32x4 mfma(u32x4 a, u32x4 b, f32x4 c) {
  return __builtin_amdgcn_mfma_f32_16x16x32_bf16(__builtin_bit_cast(bf16x8, a), __builtin_bit_cast(bf16x8, b), c, 0, 0, 0);
}
__device__ __forceinline__ u32x4 diag(u32 vv, const u32x4& msk) { return u32x4{vv & msk[0], vv & msk[1], vv & msk[2], vv & msk[3]}; }
template <int R> __device__ __forceinline__ u32 row_bcast(u32 v) { return (u32)__builtin_amdgcn_mov_dpp((int)v, 0x150 + R, 0xf, 0xf, true); }
template <int R> __device__ __forceinline__ u32 quad_bcast(u32 v) { return (u32)__builtin_amdgcn_mov_dpp((int)v, R * 0x55, 0xf, 0xf, true); }
__device__ __forceinline__ f32x4 pair_sum(const u32x4 (&v)[4], const u32x4& sel) {
  const f32x4 z = {0.f, 0.f, 0.f, 0.f};
  return mfma(v[3], sel, mfma(v[2], sel, mfma(v[1], sel, mfma(v[0], sel, z))));
}
__device__ __forceinline__ float pair_fin(const f32x4& a, int lane) {
  float t = (lane & 4) ? a[1] + a[3] : a[0] + a[2];
  t = x16_sum(t, t);
  return x32_sum(t, t);
}

__device__ __forceinline__ u32 now() { return (u32)__builtin_amdgcn_s_memrealtime(); }
__device__ __forceinline__ void wait_time(u32 t) {
  SB();
#pragma nounroll
  for (int spin = 0; spin < 4096; ++spin) if ((int)(now() - t) >= 0) break;
  SB();
}
__device__ __forceinline__ float since(u32 t, u32 tt, u32 tag, u32 tq) { return tt == tag ? (float)(int)(t - tq) : 0.f; }
__device__ __forceinline__ u32 latest(float d, u32 tq) {
  d = fmaxf(d, dpp<XOR1>(d)); d = fmaxf(d, dpp<XOR2>(d)); d = fmaxf(d, dpp<XOR7>(d)); d = fmaxf(d, dpp<ROR8>(d)); d = x32_max(x16_max(d));
  return tq + min(max(__builtin_amdgcn_readfirstlane((int)d), 0), 300);
}
__device__ __forceinline__ u32 latest(const u32x4& a, const u32x4& b, u32 tag, u32 tq) {
  return latest(fmaxf(fmaxf(since(a[0], a[1], tag, tq), since(a[2], a[3], tag, tq)), fmaxf(since(b[0], b[1], tag, tq), since(b[2], b[3], tag, tq))), tq);
}
__device__ __forceinline__ void bump(u32* c, int lane) { PIN(); if (lane == 0) __hip_atomic_fetch_add(c, 1u, __ATOMIC_RELAXED, __HIP_MEMORY_SCOPE_WORKGROUP); PIN(); }
__device__ __forceinline__ void until(u32* c, u32 n) {
#pragma nounroll
  for (int spin = 0; spin < SPIN_CAP * 16; ++spin) { PIN(); if (__builtin_amdgcn_readfirstlane(*(volatile LAS u32*)c) >= n) break; }
  PIN();
}


__global__ void __launch_bounds__(NT, 2)
fused_block(const u16*, const u16*, const u16*, const u16*, const u16*, const u16*, const u16*, const u16*, u16*, unsigned char*) {
  __shared__ __attribute__((aligned(16))) u32 lds[4096 + 1024 + 7 * 576];
  u32x4* ptab = (u32x4*)lds;
  u32* qs = lds + 4096;
  float* sm = (float*)(lds + 4352);
  float* sl = (float*)(lds + 4368);
  u32* cnt = lds + 4404;
  float* mst = (float*)(lds + 4608);
  u16* w1t = (u16*)(lds + 5120);
  const int tid = threadIdx.x, lane = tid & 63, w = __builtin_amdgcn_readfirstlane(tid >> 6);
  const int bid = blockIdx.x, g = bid & 7, c = bid >> 3;
  const int l16 = lane & 15, lg = lane >> 4;
  u64 tq64;
  {
    u64 pc; u32 d0, d1, d2, d3;
    asm volatile("s_getpc_b64 %1\n\ts_cmp_lg_u32 %6, 1\n\ts_cbranch_scc1 1f\n\t"
                 "s_load_dword %2, %1, %7\n\ts_load_dword %3, %1, %7 offset:0x80\n\ts_load_dword %4, %1, %7 offset:0x100\n\ts_load_dword %5, %1, %7 offset:0x180\n\t"
                 "s_barrier\n\ts_barrier\n\ts_memrealtime %0\n\ts_waitcnt lgkmcnt(0)\n1:"
                 : "=&s"(tq64), "=&s"(pc), "=&s"(d0), "=&s"(d1), "=&s"(d2), "=&s"(d3) : "s"(w), "s"(min(c * 512, CODE_PF - 512)) : "memory", "scc");
  }
  const __attribute__((address_space(4))) u64* ka = (const __attribute__((address_space(4))) u64*)__builtin_amdgcn_kernarg_segment_ptr();
  asm volatile("" : "+s"(ka));
#define ARG(T, i) ((T*)(GAS T*)ka[i])
  const u16* __restrict__ x = ARG(const u16, 0); const u16* __restrict__ kc = ARG(const u16, 1); const u16* __restrict__ vc = ARG(const u16, 2);
  const u16* __restrict__ wq = ARG(const u16, 3); const u16* __restrict__ wo = ARG(const u16, 4); const u16* __restrict__ w1 = ARG(const u16, 5);
  const u16* __restrict__ b1 = ARG(const u16, 6); const u16* __restrict__ w2 = ARG(const u16, 7);
  u16* __restrict__ out = ARG(u16, 8); unsigned char* __restrict__ ws_ = ARG(unsigned char, 9);
  asm volatile("" :: "s"(x), "s"(kc), "s"(vc), "s"(wq), "s"(wo), "s"(w1), "s"(b1), "s"(w2), "s"(out), "s"(ws_));
  const rsrc_t ws = __builtin_amdgcn_make_buffer_rsrc(ws_, 0, B_END, 0x00020000);
  const int key0 = g * SK + (SK - WIN) + c * 64;
  const int x4 = XROT4, x3 = XROT3;
  const bool is_mlp = bid >= MLP0 && bid < MLP0 + NMLP;
  const int mj = g * 14 + ((bid - MLP0) >> 3);
  const f32x4 zero4 = {0.f, 0.f, 0.f, 0.f};
  constexpr float LOG2E = 1.4426950408889634f;
#define TAG(e) (((e) * 0x9E3779B1u + 0x7F4A7C15u) & 0xBFFFBFFFu | 2u)

  u32x2 tgv;
  u32 tag;
  u32x4 wor[8];
#define WO_LOAD() do { \
    const rsrc_t ro = __builtin_amdgcn_make_buffer_rsrc((void*)wo, 0, QD * D * 2, 0x00020000); \
    const int wv = (w < 3 || l16 < 8) ? ((g * 512 + (c >> 2) * 128 + (c & 1) * 64 + (c & 2) * 2 + lg) * D + w * 128 + l16 * 8) * 2 : OOR; \
    _Pragma("unroll") for (int r = 0; r < 8; ++r) { wor[r] = __builtin_amdgcn_raw_buffer_load_b128(ro, wv, r * 8 * D * 2, KV_NT ? 2 : 0); SB(); } } while (0)
  if (w == 0) {
    __builtin_amdgcn_s_setprio(3);
    const int row0 = (c >> 3) * 112 + (lane >> 3) * 14;
    const u32x4 xr = ld16(x + (lane < 56 ? lane : 0) * 8);
    const u32x4 xa = *(const GAS u32x4a*)(x + row0);
    const u32x3 xb = *(const GAS u32x3a*)(x + row0 + 8);
    const u32 ep = __builtin_amdgcn_raw_buffer_load_b32(ws, B_EPOCH, 0, EP_AUX);
    SB();
    u32x4 wr[14];
    {
      const rsrc_t rq = __builtin_amdgcn_make_buffer_rsrc((void*)wq, 0, D * QD * 2, 0x00020000);
      const int vo = (row0 * QD + g * 512 + (c & 7) * 64 + (lane & 7) * 8) * 2;
#pragma unroll
      for (int t = 0; t < 14; ++t) { wr[t] = __builtin_amdgcn_raw_buffer_load_b128(rq, vo, t * QD * 2, KV_NT ? 2 : 0); SB(); }
    }
    __builtin_amdgcn_s_barrier();
    u32x4 se, so;
#pragma unroll
    for (int r = 0; r < 4; ++r) {
      const bool on = ((lane & 7) >> 1) == r;
      se[r] = on ? ((lane & 1) ? 0x01000c0cu : 0x0c0c0100u) : 0x0c0c0c0cu;
      so[r] = on ? ((lane & 1) ? 0x03020c0cu : 0x0c0c0302u) : 0x0c0c0c0cu;
    }
    float xs[8];
#pragma unroll
    for (int k = 0; k < 4; ++k) { xs[2 * k] = bf_lo(xr[k]); xs[2 * k + 1] = bf_hi(xr[k]); }
    float s = ((xs[0] + xs[1]) + (xs[2] + xs[3])) + ((xs[4] + xs[5]) + (xs[6] + xs[7]));
    if (lane >= 56) s = 0.f;
    const float mean = wave_sum(s) * (1.f / D);
    float v = 0.f;
#pragma unroll
    for (int k = 0; k < 8; ++k) { const float d = xs[k] - mean; v += d * d; }
    if (lane >= 56) v = 0.f;
    const float var = wave_sum(v) * (1.f / D);
    const float rden = __builtin_amdgcn_rcpf(bfr(__builtin_amdgcn_sqrtf(bfr(bfr(var) + 1e-5f))));
    const float mb = bfr(mean);
    const u32 xp[7] = {xa[0], xa[1], xa[2], xa[3], xb[0], xb[1], xb[2]};
    u32x4 qa[14];
#pragma unroll
    for (int k = 0; k < 7; ++k) {
      const u32 dd = pk(bf_lo(xp[k]) - mb, bf_hi(xp[k]) - mb);
      const u32 nn = pk(bf_lo(dd) * rden, bf_hi(dd) * rden);
#pragma unroll
      for (int r = 0; r < 4; ++r) { qa[2 * k][r] = __builtin_amdgcn_perm(nn, nn, se[r]); qa[2 * k + 1][r] = __builtin_amdgcn_perm(nn, nn, so[r]); }
    }
    tag = TAG(ep);
    if (lane == 0) lds[4400] = tag;
    f32x4 q0 = zero4;
#pragma unroll
    for (int t = 0; t < 14; ++t) q0 = mfma(qa[t], wr[t], q0);
    {
      auto s02 = __builtin_amdgcn_permlane32_swap(asu(q0[0]), asu(q0[2]), false, false);
      auto s13 = __builtin_amdgcn_permlane32_swap(asu(q0[1]), asu(q0[3]), false, false);
      const float u0 = asf(s02[0]) + dpp<ROR8>(asf(s02[1])), u1 = asf(s13[0]) + dpp<ROR8>(asf(s13[1]));
      if (l16 < 8) {
        const int oq = B_Q + g * 8192 + c * 256 + (l16 * 4 + (lg & 1) * 2 + (lg >> 1)) * 8;
        const u32 qq = pk(u0, u1);
        put1(ws, oq, tag, qq); put1(ws, oq + QC, tag, qq);
      }
    }
    BAR();
    if (bid >= NCON) goto done;
    until(cnt + 2, 2);
    if (WO_Q) until(cnt + 1, 4);
    get(ws, B_T + (g * 32 + (lane & 31)) * 8, tgv);
    WO_LOAD();
    BAR();
  } else {
    if (w >= 2 && w < 6) {
      const int kw = w - 2;
#pragma unroll
      for (int k = 0; k < 4; ++k) ptab[kw * 256 + k * 64 + lane] = u32x4{0u, 0u, 0u, 0u};
      if (tid < 128 + 4) cnt[lane] = 0;
      u32x4 kr[8];
      const rsrc_t rk = __builtin_amdgcn_make_buffer_rsrc((void*)kc, 0, 8 * SK * DH * 2, 0x00020000);
      const int kp = (key0 + ((kw * 16 + l16) ^ (x4 * 4))) * (DH * 2) + lg * 16;
      asm volatile("" :: "v"(kp));
      __builtin_amdgcn_s_barrier();
      nap<D_K>();
      SB();
#pragma unroll
      for (int s = 0; s < K_EARLY; ++s) { kr[s] = __builtin_amdgcn_raw_buffer_load_b128(rk, kp, (s ^ x3) * 64, KV_NT ? 2 : 0); SB(); }
      BAR();
#pragma unroll
      for (int s = K_EARLY; s < K_MID; ++s) { kr[s] = __builtin_amdgcn_raw_buffer_load_b128(rk, kp, (s ^ x3) * 64, KV_NT ? 2 : 0); SB(); }
      tag = lds[4400];
      nap<D_Q>();
      {
        u32x2 qv[4];
        const int o0 = B_Q + g * 8192 + (kw * 64 + lane) * 8;
        const int off[4] = {o0, o0 + 2048, o0 + 4096, o0 + 6144};
        const u32 tg[4] = {tag, tag, tag, tag};
        poll_issue<4>(ws, off, qv);
#pragma unroll
        for (int s = K_MID; s < 8; ++s) { kr[s] = __builtin_amdgcn_raw_buffer_load_b128(rk, kp, (s ^ x3) * 64, KV_NT ? 2 : 0); SB(); }
        bump(cnt, lane);
        const int of2[4] = {o0 + QC, o0 + QC + 2048, o0 + QC + 4096, o0 + QC + 6144};
        poll_wait<4>(ws, of2, tg, qv);
        PIN();
        qs[(kw * 64 + lane) ^ (x3 << 4)] = pk((bf_lo(qv[0][0]) + bf_lo(qv[1][0])) + (bf_lo(qv[2][0]) + bf_lo(qv[3][0])),
                                (bf_hi(qv[0][0]) + bf_hi(qv[1][0])) + (bf_hi(qv[2][0]) + bf_hi(qv[3][0])));
      }
      bump(cnt + 1, lane);
      f32x4 s0 = zero4;
      const u32* qp = qs + (l16 >> 3) * 128 + lg * 4;
      u32x4 qb[8];
#pragma nounroll
      for (int spin = 0; spin < SPIN_CAP * 16; ++spin) {
        PIN();
        const u32 n = *(volatile LAS u32*)(cnt + 1);
        PIN();
#pragma unroll
        for (int s = 0; s < 8; ++s) qb[s] = *(const u32x4*)(qp + s * 16);
        SB();
        if (__builtin_amdgcn_readfirstlane(n) >= 4) break;
      }
      SB();
#pragma unroll
      for (int s = 0; s < 8; ++s) s0 = mfma(kr[s], qb[s], s0);
      constexpr float C2 = 0.0625f * LOG2E;
      const u32 r01 = pk(s0[0], s0[1]), r23 = pk(s0[2], s0[3]);
      const float c0 = bf_lo(r01), c1 = bf_hi(r01), c2 = bf_lo(r23), c3 = bf_hi(r23);
      const float m = x32_max(x16_max(fmaxf(fmaxf(c0, c1), fmaxf(c2, c3)))) * C2;
      const float p0 = __builtin_amdgcn_exp2f(c0 * C2 - m), p1 = __builtin_amdgcn_exp2f(c1 * C2 - m);
      const float p2 = __builtin_amdgcn_exp2f(c2 * C2 - m), p3 = __builtin_amdgcn_exp2f(c3 * C2 - m);
      const u32 p01 = pk(p0, p1), p23 = pk(p2, p3);
      u16* pt = (u16*)(ptab + (kw * 4 + lg) * 64 + l16) + (lane & 7);
      pt[0] = (u16)p01; pt[128] = (u16)(p01 >> 16); pt[256] = (u16)p23; pt[384] = (u16)(p23 >> 16);
      if ((lane & 7) == 0) { sl[(l16 >> 3) * 16 + kw * 4 + lg] = (p0 + p1) + (p2 + p3); sm[(l16 >> 3) * 4 + kw] = m; }
      BAR();
      if (WO_B3 && bid < NCON && w < 4) { nap<D_WO>(); WO_LOAD(); }
    } else if (w >= 6) {
#define VT(t) (t)
      u32x4 vr[16];
      const rsrc_t rv = __builtin_amdgcn_make_buffer_rsrc((void*)vc, 0, 8 * SK * DH * 2, 0x00020000);
      const int vp = (key0 + lg) * (DH * 2) + (w - 6) * 256 + l16 * 16;
      asm volatile("" :: "v"(vp));
      __builtin_amdgcn_s_barrier();
#define VLD(t) do { vr[t] = __builtin_amdgcn_raw_buffer_load_b128(rv, vp, (VT(t) ^ x4) * 2048, KV_NT ? 2 : 0); SB(); } while (0)
      nap<D_V>();
#pragma unroll
      for (int t = 0; t < V_FIRST; ++t) VLD(t);
      BAR();
      tag = lds[4400];
#pragma unroll
      for (int t = V_FIRST; t < V_EARLY; ++t) VLD(t);
      until(cnt, 4);
#pragma unroll
      for (int t = V_EARLY; t < 16; ++t) VLD(t);
      bump(cnt + 2, lane);
      BAR();
      const f32x4 mw = *(const f32x4*)(sm + (lane >> 5) * 4);
      u32x4 pa[16];
#pragma unroll
      for (int t = 0; t < 16; ++t) pa[t] = ptab[VT(t) * 64 + lane];
      SB();
      f32x4 oa[4];
#pragma unroll
      for (int t = 0; t < 16; ++t) oa[t >> 2] = mfma(pa[t], vr[t], (t & 3) ? oa[t >> 2] : zero4);
      const float m = fmaxf(fmaxf(mw[0], mw[1]), fmaxf(mw[2], mw[3]));
      const float f0 = __builtin_amdgcn_exp2f(mw[0] - m), f1 = __builtin_amdgcn_exp2f(mw[1] - m);
      const float f2 = __builtin_amdgcn_exp2f(mw[2] - m), f3 = __builtin_amdgcn_exp2f(mw[3] - m);
      const f32x4 o = oa[3] * f3 + (oa[2] * f2 + (oa[1] * f1 + oa[0] * f0));
      put2(ws, B_O + (g * 32 + c) * 2048 + (lane >> 5) * 1024 + (w - 6) * 512 + (lane & 31) * 16, tag, pk(o[0], o[1]), pk(o[2], o[3]));
    } else {
      const u32 tq = (u32)tq64;
      tag = lds[4400];
      lds[4401] = tq;
      if (lane == 0) put1(ws, B_T + (g * 32 + c) * 8, tag, tq);
      asm volatile("s_waitcnt vmcnt(0)" ::: "memory");
      if (WO_EW > 1 && bid < NCON) { until(cnt + 2, 2); if (WO_Q) until(cnt + 1, 4); WO_LOAD(); }
      BAR();
      const float mw = sm[(lg & 1) * 4 + (l16 >> 2)], lw = sl[lane & 31];
      float m = fmaxf(mw, dpp<ROR4>(mw)); m = fmaxf(m, dpp<ROR8>(m));
      const float l = row_sum(lw * __builtin_amdgcn_exp2f(mw - m));
      if (lane == 0 || lane == 16) put2(ws, B_ML + ((g * 2 + lg) * 32 + c) * 16, tag, asu(m), asu(l));
    }
  }
  __builtin_amdgcn_s_barrier();
  {
    const u32 tq = __builtin_amdgcn_readfirstlane(lds[4401]);
    u32x4 msk;
#pragma unroll
    for (int r = 0; r < 4; ++r) msk[r] = ((lane & 7) >> 1) == r ? ((lane & 1) ? 0xffff0000u : 0x0000ffffu) : 0u;
    if (bid < NCON) {
      if (w >= 4) goto done;
      const bool colok = w < 3 || l16 < 8;
      if (w >= WO_EW && (!WO_B3 || w < 2)) WO_LOAD();
      u32* osh = lds + 4416;
      int oh = (colok && lane < 32) ? B_HP + (w * 16 + (lane >> 1)) * (NCON * 32) + (g * 16 + c) * 32 + (lane & 1) * 16 : OOR;
      int ord = lg * 32;
      u32 me = (lane & 1) ? 0xffff0000u : 0x0000ffffu, m0 = (lane & 2) ? 0u : me, m2 = (lane & 2) ? me : 0u;
      u32 tg[4] = {tag, tag, tag, tag};
      u32 tl = tq;
#pragma unroll
      for (int k = 0; k < 4; ++k) KEEP(msk[k]);
      KEEP(oh); KEEP(ord);
      if (w == 0) {
        int offm[1] = {B_ML + ((g * 2 + (c >> 3)) * 32 + (lane & 3) * 8 + lg * 2 + ((lane >> 2) & 1)) * 16};
        int offo[4], owr = (l16 < 8 ? lg * 8 + l16 : 32 + lane) * 4;
        u32 tg1[1] = {tag};
#pragma unroll
        for (int k = 0; k < 4; ++k) { offo[k] = B_O + (g * 32 + k * 8 + (lane >> 3)) * 2048 + c * 128 + (lane & 7) * 16; KEEP(offo[k]); }
        KEEP(offm[0]); KEEP(m0); KEEP(m2); KEEP(owr);
        const u32 t0 = latest(since(tgv[0], tgv[1], tag, tq), tq);
        u32x4 tv, tw;
        get(ws, B_T + lane * 32, tv); get(ws, B_T + lane * 32 + 16, tw);
        u32x4 ml[1], ov[4];
        wait_time(t0 + T_O - T_ML);
        poll_issue<1>(ws, offm, ml);
        wait_time(t0 + T_O);
        poll_issue<4>(ws, offo, ov);
        tl = latest(tv, tw, tag, tq);
        SB();
        poll_wait<1>(ws, offm, tg1, ml);
        float M = asf(ml[0][0]);
        M = fmaxf(M, dpp<XOR1>(M)); M = fmaxf(M, dpp<XOR2>(M)); M = fmaxf(M, dpp<XOR7>(M)); M = x32_max(x16_max(M));
        const float ea = __builtin_amdgcn_exp2f(asf(ml[0][0]) - M);
        float L = ea * asf(ml[0][2]);
        L += dpp<XOR1>(L); L += dpp<XOR2>(L); L += dpp<XOR7>(L); L = x16_sum(L, L); L = x32_sum(L, L);
        const float wa = ea * __builtin_amdgcn_rcpf(L);
        const float wl = (lane & 8) ? wa - bfr(wa) : wa;
        const u32 ww = pk(wl, wl);
        u32x4 c0 = {quad_bcast<0>(ww) & m0, 0u, quad_bcast<0>(ww) & m2, 0u}, c1 = {quad_bcast<1>(ww) & m0, 0u, quad_bcast<1>(ww) & m2, 0u};
        u32x4 c2 = {quad_bcast<2>(ww) & m0, 0u, quad_bcast<2>(ww) & m2, 0u}, c3 = {quad_bcast<3>(ww) & m0, 0u, quad_bcast<3>(ww) & m2, 0u};
        KEEP(c0); KEEP(c1); KEEP(c2); KEEP(c3);
        SB();
        f32x4 e;
        poll_wait<4>(ws, offo, tg, ov, [&] { e = mfma(c3, ov[3], mfma(c2, ov[2], mfma(c1, ov[1], mfma(c0, ov[0], zero4)))); });
        auto sw = __builtin_amdgcn_permlane16_swap(asu(x32_sum(e[0], e[2])), asu(x32_sum(e[1], e[3])), false, false);
        const float o = asf(sw[0]) + dpp<ROR8>(asf(sw[1]));
        *(u32*)((char*)osh + owr) = pk(o, o);
      }
#pragma unroll
      for (int r = 0; r < 8; ++r) KEEP(wor[r]);
      BAR();
      {
        const u32x4 oa = *(const u32x4*)((char*)osh + ord), ob = *(const u32x4*)((char*)osh + ord + 16);
        SB();
        f32x4 acc = zero4;
        u32x4 da[8];
#pragma unroll
        for (int r = 0; r < 8; ++r) { da[r] = diag(r < 4 ? oa[r & 3] : ob[r & 3], msk); acc = mfma(da[r], wor[r], acc); }
#pragma unroll
        for (int r = 0; r < 8; ++r) asm volatile("" :: "v"(da[r]), "v"(wor[r]), "v"(acc));
        put2(ws, oh, tag, pk(acc[0], acc[1]), pk(acc[2], acc[3]));
      }
      if (bid < NRED && w == 0) {
        const int r = bid < 52 ? bid : bid + 4;
        const int col = (r >> 4) * 128 + (r & 7) * 16 + ((lane >> 2) & 1) * 8 + ((r >> 3) & 1) * 4 + (lane & 3);
        float xh = bf_lo(x[col]);
        u32x4 sel = {m0 & 0x3f803f80u, 0u, m2 & 0x3f803f80u, 0u};
        int off[4], ohh = (lane & 3) == 0 ? B_H + (lane >> 3) * 2048 + bid * 32 + (lane & 4) * 4 : (lane & 0x3b) == 1 ? B_H + 8 * 2048 + bid * 32 + (lane & 4) * 4 : OOR;
#pragma unroll
        for (int k = 0; k < 4; ++k) off[k] = B_HP + r * (NCON * 32) + k * 1024 + lane * 16;
        KEEP(sel); KEEP(xh); KEEP(ohh); KEEP(off[0]);
        nap<D_HP>();
        wait_time(tl + T_HP);
        u32x4 v4[4];
        poll_issue<4>(ws, off, v4);
        f32x4 ps;
        poll_wait<4>(ws, off, tg, v4, [&] { ps = pair_sum(v4, sel); });
        const float hv = bfr(xh + bfr(pair_fin(ps, lane)));
        {
          const u32 hb = asu(hv);
          const u32 p01 = __builtin_amdgcn_perm(quad_bcast<1>(hb), quad_bcast<0>(hb), 0x07060302u), p23 = __builtin_amdgcn_perm(quad_bcast<3>(hb), quad_bcast<2>(hb), 0x07060302u);
          put2(ws, ohh, tag, p01, p23);
        }
        const int o0 = B_OUT + r * (NMLP * 32) + lane * 16;
        int of2[4] = {o0, o0 + 1024, o0 + 2048, lane < 32 ? o0 + 3072 : OOR};
        u32 tg2[4] = {tag, tag, tag, lane < 32 ? tag : 0u};
        const rsrc_t rout = __builtin_amdgcn_make_buffer_rsrc((void*)out, 0, D * 2, 0x00020000);
        int oc = lane < 8 ? col * 2 : OOR;
        KEEP(of2[0]); KEEP(of2[3]); KEEP(tg2[3]); KEEP(oc);
        {
          u32x4 hx[1];
          const int ofh[1] = {lane < 56 ? B_H + 8 * 2048 + lane * 32 : OOR};
          const u32 tgh[1] = {lane < 56 ? tag : 0u};
          nap<D_HR>();
          poll_issue<1>(ws, ofh, hx);
          poll_wait<1>(ws, ofh, tgh, hx);
        }
        nap<D_OUT>();
        wait_time(tl + T_OUT);
        u32x4 u4[4];
        poll_issue<4>(ws, of2, u4);
        poll_wait<4>(ws, of2, tg2, u4, [&] { ps = pair_sum(u4, sel); });
        const float ov = hv + bfr(pair_fin(ps, lane));
        __builtin_amdgcn_raw_buffer_store_b16((u16)pk(ov, ov), rout, oc, 0, 0);
        if (bid == 0 && lane == 0) __builtin_amdgcn_raw_buffer_store_b32(tag, ws, B_EPOCH, 0, 16);
      }
    } else if (is_mlp) {
      const int mw = w - 1;
      u32x4 wa[2], w2r[4];
      nap<D_W1>();
#pragma unroll
      for (int k = 0; k < 2; ++k) {
        const int u = k * 32 + (lane >> 1);
        const int row = mw < 6 ? (mw >> 1) * 128 + (u >> 2) * 8 + (mw & 1) * 4 + (u & 3) : 384 + ((u >> 2) & 7) * 8 + (u >> 5) * 4 + (u & 3);
        wa[k] = ld16(w1 + (size_t)row * FF + mj * 16 + (lane & 1) * 8);
      }
      const int n = lg * 4 + (lane & 3);
      const u32 braw = b1[mj * 16 + n];
      const int ch = mw & 3;
      const bool colok = ch < 3 || l16 < 8;
      if (mw < 4) {
        const u16* wp2 = w2 + (size_t)(mj * 16 + lg * 4) * D + (colok ? ch * 128 + l16 * 8 : 0);
#pragma unroll
        for (int r = 0; r < 4; ++r) w2r[r] = ld16nt(wp2 + r * D);
      }
      u32x4 tv, tw;
      get(ws, B_T + lane * 32, tv); get(ws, B_T + lane * 32 + 16, tw);
      SB();
      u16* wt = w1t + mw * (16 * 72);
#pragma unroll
      for (int k = 0; k < 2; ++k)
#pragma unroll
        for (int j = 0; j < 4; ++j) {
          u16* p = wt + ((lane & 1) * 8 + 2 * j) * 72 + k * 32 + (lane >> 1);
          p[0] = (u16)wa[k][j]; p[72] = (u16)(wa[k][j] >> 16);
        }
      asm volatile("s_waitcnt lgkmcnt(0)" ::: "memory");
      const u32x4 b0 = *(const u32x4*)(wt + l16 * 72 + lg * 8), b1f = *(const u32x4*)(wt + l16 * 72 + 32 + lg * 8);
      u32x4 ones = {0x3f803f80u, 0x3f803f80u, 0x3f803f80u, 0x3f803f80u};
      const f32x4 cs = mfma(ones, b1f, mfma(ones, b0, zero4));
      if (lane < 16) { mst[128 + lane * 8 + mw] = cs[0]; if (mw == 0) { mst[128 + lane * 8 + 7] = 0.f; mst[lane * 8 + 7] = 0.f; mst[256 + 14 + (lane & 1)] = 0.f; } }
      BAR();
      const f32x2 cc = *(const f32x2*)(mst + 128 + n * 8 + (l16 >> 2) * 2);
      float C = cc[0] + cc[1];
      C += dpp<ROR4>(C); C += dpp<ROR8>(C);
      float bias = bf_lo(braw);
      int ws_a = (lane < 16 ? lane * 8 + mw : 272 + lane) * 4, ws_b = (lane == 0 ? 256 + mw * 2 : 336 + lane * 2) * 4;
      int rs_a = (n * 8 + (l16 >> 2) * 2) * 4, rs_b = (256 + (l16 >> 2) * 4) * 4;
      int oo = (colok && lane < 32) ? B_OUT + (ch * 16 + (lane >> 1)) * (NMLP * 32) + mj * 32 + (lane & 1) * 16 : OOR;
      int off[2];
      off[0] = l16 < 2 ? B_H + g * 2048 + (mw * 8 + l16 * 4 + lg) * 32 : OOR - 16;
      off[1] = off[0] + 16;
      u32 tg[2] = {l16 < 2 ? tag : 0u, l16 < 2 ? tag : 0u};
#pragma unroll
      for (int r = 0; r < 4; ++r) { KEEP(msk[r]); KEEP(ones[r]); }
      KEEP(C); KEEP(bias); KEEP(ws_a); KEEP(ws_b); KEEP(rs_a); KEEP(rs_b); KEEP(oo); KEEP(off[0]); KEEP(tg[0]);
      tg[1] = tg[0];
      asm volatile("s_waitcnt vmcnt(0)" ::: "memory");
      wait_time(latest(tv, tw, tag, tq) + T_H);
      u32x4 hv[2];
      poll_issue<2>(ws, off, hv);
      f32x4 sa, sb, s1, s2;
      poll_wait<2>(ws, off, tg, hv, [&] {
        const u32x4 ha = {hv[0][0], hv[0][2], hv[1][0], hv[1][2]};
        s2 = mfma(ha, ha, zero4); s1 = mfma(ha, ones, zero4); sa = mfma(ha, b0, zero4); sb = mfma(ha, b1f, zero4);
      });
      *(f32x2*)((char*)mst + ws_b) = f32x2{s1[0] + s1[1], s2[0] + dpp<XOR1>(s2[1])};
      *(float*)((char*)mst + ws_a) = sa[0] + sb[1];
      BAR();
      if (mw < 4) {
        const f32x2 a2 = *(const f32x2*)((char*)mst + rs_a);
        const f32x4 t4 = *(const f32x4*)((char*)mst + rs_b);
        SB();
        float S = a2[0] + a2[1], mean = t4[0] + t4[2], ex2 = t4[1] + t4[3];
        S += dpp<ROR4>(S); mean += dpp<ROR4>(mean); ex2 += dpp<ROR4>(ex2);
        S += dpp<ROR8>(S); mean += dpp<ROR8>(mean); ex2 += dpp<ROR8>(ex2);
        mean *= 1.f / D;
        const float rstd = __builtin_amdgcn_rsqf(fmaxf(ex2 * (1.f / D) - mean * mean, 0.f) + 1e-5f);
        const float pre = rstd * (S - mean * C) + bias;
        const float u = __builtin_amdgcn_exp2f(fminf(pre * LOG2E, 43.f));
        const float num = u * (u + 2.f);
        const float act = pre * num * __builtin_amdgcn_rcpf(num + 2.f);
        const u32 aa = pk(act, act);
        const u32x4 d0 = diag(quad_bcast<0>(aa), msk), d1 = diag(quad_bcast<1>(aa), msk), d2 = diag(quad_bcast<2>(aa), msk), d3 = diag(quad_bcast<3>(aa), msk);
        const f32x4 a = mfma(d3, w2r[3], mfma(d2, w2r[2], mfma(d1, w2r[1], mfma(d0, w2r[0], zero4))));
        asm volatile("" :: "v"(d0), "v"(d1), "v"(d2), "v"(d3), "v"(w2r[0]), "v"(w2r[1]), "v"(w2r[2]), "v"(w2r[3]), "v"(a));
        put2(ws, oo, tag, pk(a[0], a[1]), pk(a[2], a[3]));
      }
    }
  }
done:;
}

extern "C" void kernel_launch(void* const* d_in, const int* in_sizes, int n_in, void* d_out, int out_size, void* d_ws, size_t ws_size,
                              hipStream_t stream) {
  fused_block<<<NWG, NT, 0, stream>>>((const u16*)d_in[0], (const u16*)d_in[1], (const u16*)d_in[2], (const u16*)d_in[3], (const u16*)d_in[4],
                                      (const u16*)d_in[6], (const u16*)d_in[7], (const u16*)d_in[8], (u16*)d_out, (unsigned char*)d_ws);
}
```
